```python
import torch

def _softmax(x, axis=-1):
    return torch.softmax(x, dim=axis)
def _gelu(x):
    return torch.nn.functional.gelu(x, approximate="tanh")
def _silu(x):
    return torch.nn.functional.silu(x)
def _sigmoid(x):
    return torch.sigmoid(x)
def _layernorm(x, axis=-1, eps=1e-5):
    m = x.mean(dim=axis, keepdim=True)
    v = x.var(dim=axis, keepdim=True, unbiased=False)
    return (x - m) / torch.sqrt(v + eps)
def _scatter_add(tgt, idx, src, axis=0):
    return tgt.clone().index_add_(axis, idx, src)
def _segment_sum(src, seg, n):
    out = torch.zeros((n,) + src.shape[1:], dtype=src.dtype)
    return out.index_add_(0, seg, src)
def _pad(x, pw):
    flat = tuple(v for pair in reversed(pw) for v in pair)
    return torch.nn.functional.pad(x, flat)

kernel_name = 'concat_heads__compressed_kv_cla__ssm_conv_s4__prompt'

def setup_inputs():

    _w = (lambda a: (lambda t: t.to(torch.bfloat16) if t.is_floating_point() else t)(torch.as_tensor(a)))
    def _A_setup():
        B, H, Sq, Sk, D, M = 1, 32, 2048, 2048, 128, 512
        Dc = 4 * D
        return {
            "x": _w((lambda *s: torch.randn(*s, dtype=torch.bfloat16) * 3.2)(B, Sq, M)),
            "c_kv": _w((lambda *s: torch.randn(*s, dtype=torch.bfloat16) * 0.1)(B, Sk, Dc)),
            "w_q": _w((lambda *s: torch.randn(*s, dtype=torch.bfloat16) * 6.4)(M, H, D) * (M ** -0.5)),
            "w_uk": _w((lambda *s: torch.randn(*s, dtype=torch.bfloat16) * 6.4)(Dc, H, D) * (Dc ** -0.5)),
            "w_uv": _w((lambda *s: torch.randn(*s, dtype=torch.bfloat16) * 0.1)(Dc, H, D) * (Dc ** -0.5)),
            "w_o": _w((lambda *s: torch.randn(*s, dtype=torch.bfloat16) * 0.1)(H, D, M) * ((H * D) ** -0.5)),
        }
    _A_in = _A_setup()

    def _B_setup():
        B, L, D, N = 1, 2048, 512, 128
        A_re = -(lambda _x: torch.exp(torch.as_tensor(_x)))((lambda *s: torch.randn(*s, dtype=torch.bfloat16) * 3.2)(D, N))
        A_im = torch.pi * (lambda *s: torch.randn(*s, dtype=torch.bfloat16) * 0.05)(D, N)
        B_re = (lambda *s: torch.randn(*s, dtype=torch.bfloat16) * 0.1)(D, N)
        B_im = (lambda *s: torch.randn(*s, dtype=torch.bfloat16) * 0.1)(D, N)
        C_re = (lambda *s: torch.randn(*s, dtype=torch.bfloat16) * 0.1)(D, N)
        C_im = (lambda *s: torch.randn(*s, dtype=torch.bfloat16) * 0.1)(D, N)
        log_dt = (lambda *s: torch.randn(*s, dtype=torch.bfloat16) * 0.05)(D) * 0.5 - 4.0
        Dskip = (lambda *s: torch.randn(*s, dtype=torch.bfloat16) * 0.05)(D)
        return {
            "u": _w((lambda *s: torch.randn(*s, dtype=torch.bfloat16) * 0.05)(B, L, D)),
            "A_re": _w(A_re),
            "A_im": _w(A_im),
            "B_re": _w(B_re),
            "B_im": _w(B_im),
            "C_re": _w(C_re),
            "C_im": _w(C_im),
            "log_dt": _w(log_dt),
            "Dskip": _w(Dskip),
        }
    _B_in = _B_setup()

    return {
        "A_x": _A_in["x"],
        "A_c_kv": _A_in["c_kv"],
        "A_w_q": _A_in["w_q"],
        "A_w_uk": _A_in["w_uk"],
        "A_w_uv": _A_in["w_uv"],
        "A_w_o": _A_in["w_o"],
        "B_u": _B_in["u"],
        "B_A_re": _B_in["A_re"],
        "B_A_im": _B_in["A_im"],
        "B_B_re": _B_in["B_re"],
        "B_B_im": _B_in["B_im"],
        "B_C_re": _B_in["C_re"],
        "B_C_im": _B_in["C_im"],
        "B_log_dt": _B_in["log_dt"],
        "B_Dskip": _B_in["Dskip"],
    }

def reference(A_x, A_c_kv, A_w_q, A_w_uk, A_w_uv, A_w_o, B_u, B_A_re, B_A_im, B_B_re, B_B_im, B_C_re, B_C_im, B_log_dt, B_Dskip):
    def _A_ref(x, c_kv, w_q, w_uk, w_uv, w_o):
        B, Sq, M = x.shape[0], x.shape[1], x.shape[2]
        Sk = c_kv.shape[1]
        H, D = w_q.shape[1], w_q.shape[2]
        q = torch.einsum("bqm,mhd->bhqd", x, w_q)
        k = torch.einsum("bkc,chd->bhkd", c_kv, w_uk)
        v = torch.einsum("bkc,chd->bhkd", c_kv, w_uv)
        scores = torch.einsum("bhqd,bhkd->bhqk", q, k) * (D ** -0.5)
        off = Sk - Sq
        qi = torch.arange(Sq)[:, None]
        kj = torch.arange(Sk)[None, :]
        cmask = kj <= (qi + off)
        scores = torch.where(cmask[None, None], scores, -1e9)
        p = _softmax(scores, axis=-1)
        ctx = torch.einsum("bhqk,bhkd->bhqd", p, v)
        return torch.einsum("bhqd,hdm->bqm", ctx, w_o)

    def _B_ref(u, A_re, A_im, B_re, B_im, C_re, C_im, log_dt, Dskip):
        Bsz, L, D = u.shape
        dt = torch.exp(log_dt)[:, None]
        A = A_re + 1j * A_im
        Bc = B_re + 1j * B_im
        Cc = C_re + 1j * C_im
        logdA = dt * A
        dB = dt * Bc
        l = (lambda x, dt: x.to(dt))(torch.arange(L), A_re.dtype)
        P = torch.exp(logdA[:, :, None] * l[None, None, :])
        K = torch.einsum("dn,dnl,dn->dl", Cc, P, dB).real
        L2 = 2 * L
        Kp = _pad(K, ((0, 0), (0, L)))
        up = (lambda x, *d: x.permute(*d))(u, 0, 2, 1)
        up = _pad(up, ((0, 0), (0, 0), (0, L)))
        Kf = (lambda x, axes=(-2,-1): torch.fft.rfft2(x.float(), dim=axes))(Kp[:, None, :], axes=(-2, -1))
        Uf = (lambda x, axes=(-2,-1): torch.fft.rfft2(x.float(), dim=axes))(up[:, :, None, :], axes=(-2, -1))
        Yf = Uf * Kf[None, :, :, :]
        y = (lambda x, s, axes=(-2,-1): torch.fft.irfft2(x, s=s, dim=axes).to(torch.bfloat16))(Yf, (1, L2), axes=(-2, -1))[:, :, 0, :L]
        y = (lambda x, *d: x.permute(*d))(y, 0, 2, 1) + u * Dskip[None, None, :]
        return y

    a = _A_ref(A_x, A_c_kv, A_w_q, A_w_uk, A_w_uv, A_w_o)
    b = _B_ref(B_u, B_A_re, B_A_im, B_B_re, B_B_im, B_C_re, B_C_im, B_log_dt, B_Dskip)
    return torch.concatenate([a, b], axis=-1)
```

```cpp
#include <hip/hip_runtime.h>
#include <hip/hip_bf16.h>
#include <stdint.h>
#define LQUANT 0
typedef unsigned short u16;
__device__ __forceinline__ float bf2f(u16 x){ return __uint_as_float(((unsigned)x)<<16); }
__device__ __forceinline__ u16 f2bf(float f){ unsigned u=__float_as_uint(f); u += 0x7fffu + ((u>>16)&1u); return (u16)(u>>16); }

__global__ void gemm_naive(const u16* A, const u16* B, u16* C, int M, int N, int K, int ldc){
  __shared__ float As[64][33]; __shared__ float Bs[32][65];
  int tx=threadIdx.x&15, ty=threadIdx.x>>4;
  int m0=blockIdx.y*64, n0=blockIdx.x*64;
  float acc[4][4]={};
  for(int k0=0;k0<K;k0+=32){
    for(int i=threadIdx.x;i<64*32;i+=256){ int r=i>>5,c=i&31; As[r][c]=bf2f(A[(size_t)(m0+r)*K+k0+c]); }
    for(int i=threadIdx.x;i<32*64;i+=256){ int r=i>>6,c=i&63; Bs[r][c]=bf2f(B[(size_t)(k0+r)*N+n0+c]); }
    __syncthreads();
    for(int k=0;k<32;k++){
      float a[4],b[4];
      for(int i=0;i<4;i++) a[i]=As[ty*4+i][k];
      for(int j=0;j<4;j++) b[j]=Bs[k][tx*4+j];
      for(int i=0;i<4;i++) for(int j=0;j<4;j++) acc[i][j]+=a[i]*b[j];
    }
    __syncthreads();
  }
  for(int i=0;i<4;i++) for(int j=0;j<4;j++) C[(size_t)(m0+ty*4+i)*ldc+n0+tx*4+j]=f2bf(acc[i][j]);
}
__global__ void attn_naive(const u16* q, const u16* k, const u16* v, u16* ctx){
  int h=blockIdx.y, qi=blockIdx.x; int t=threadIdx.x;
  __shared__ float qs[128]; __shared__ float s[2048]; __shared__ float red[256];
  if(t<128) qs[t]=bf2f(q[(size_t)qi*4096+h*128+t]);
  __syncthreads();
  float mx=-1e30f;
  for(int j=t;j<=qi;j+=256){
    const u16* kr=k+(size_t)j*4096+h*128; float a=0;
    for(int d=0;d<128;d++) a+=qs[d]*bf2f(kr[d]);
    a*=0.08838834764831845f; s[j]=a; mx=fmaxf(mx,a);
  }
  red[t]=mx; __syncthreads();
  for(int o=128;o>0;o>>=1){ if(t<o) red[t]=fmaxf(red[t],red[t+o]); __syncthreads(); }
  mx=red[0]; __syncthreads();
  float sum=0;
  for(int j=t;j<=qi;j+=256){ float p=__expf(s[j]-mx); s[j]=p; sum+=p; }
  red[t]=sum; __syncthreads();
  for(int o=128;o>0;o>>=1){ if(t<o) red[t]+=red[t+o]; __syncthreads(); }
  sum=red[0]; __syncthreads();
  int d=t&127, half=t>>7; float a=0;
  for(int j=half;j<=qi;j+=2) a+=s[j]*bf2f(v[(size_t)j*4096+h*128+d]);
  red[t]=a; __syncthreads();
  if(t<128) ctx[(size_t)qi*4096+h*128+t]=f2bf((red[t]+red[t+128])/sum);
}
__global__ void kgen_naive(const u16* Are,const u16* Aim,const u16* Bre,const u16* Bim,const u16* Cre,const u16* Cim,const u16* logdt,float* Kk){
  int d=blockIdx.y; int l=blockIdx.x*256+threadIdx.x;
  float dt=expf(bf2f(logdt[d]));
  float lf=(float)l;
  float acc=0;
  for(int n=0;n<128;n++){
    float ar=dt*bf2f(Are[d*128+n]), ai=dt*bf2f(Aim[d*128+n]);
    float br=dt*bf2f(Bre[d*128+n]), bi=dt*bf2f(Bim[d*128+n]);
    float cr=bf2f(Cre[d*128+n]), ci=bf2f(Cim[d*128+n]);
    float e=expf(ar*lf); float sn,cs; sincosf(ai*lf,&sn,&cs);
    float pr=e*cs, pi=e*sn;
    float xr=cr*br-ci*bi, xi=cr*bi+ci*br;
    acc+=xr*pr-xi*pi;
  }
  Kk[d*2048+l]=acc;
}
__global__ void conv_naive(const float* Kk,const u16* u,const u16* Dskip,u16* out){
  int d=blockIdx.x;
  __shared__ float ks[2048], us[2048];
  for(int i=threadIdx.x;i<2048;i+=256){ ks[i]=Kk[d*2048+i]; us[i]=bf2f(u[(size_t)i*512+d]); }
  __syncthreads();
  float ds=bf2f(Dskip[d]);
  for(int t=threadIdx.x;t<2048;t+=256){
    float a=0; for(int l=0;l<=t;l++) a+=ks[l]*us[t-l];
    out[(size_t)t*1024+512+d]=f2bf(a+us[t]*ds);
  }
}
extern "C" void kernel_launch(void* const* d_in, const int* in_sizes, int n_in, void* d_out, int out_size, void* d_ws, size_t ws_size, hipStream_t stream){
  const u16* x=(const u16*)d_in[0]; const u16* c=(const u16*)d_in[1]; const u16* wq=(const u16*)d_in[2]; const u16* wk=(const u16*)d_in[3];
  const u16* wv=(const u16*)d_in[4]; const u16* wo=(const u16*)d_in[5]; const u16* u=(const u16*)d_in[6];
  u16* out=(u16*)d_out;
  u16* q=(u16*)d_ws; u16* k=q+2048*4096; u16* v=k+2048*4096; u16* ctx=v+2048*4096; float* Kk=(float*)(ctx+2048*4096);
  gemm_naive<<<dim3(64,32),256,0,stream>>>(x,wq,q,2048,4096,512,4096);
  gemm_naive<<<dim3(64,32),256,0,stream>>>(c,wk,k,2048,4096,512,4096);
  gemm_naive<<<dim3(64,32),256,0,stream>>>(c,wv,v,2048,4096,512,4096);
  attn_naive<<<dim3(2048,32),256,0,stream>>>(q,k,v,ctx);
  gemm_naive<<<dim3(8,32),256,0,stream>>>(ctx,wo,out,2048,512,4096,1024);
  kgen_naive<<<dim3(8,512),256,0,stream>>>((const u16*)d_in[7],(const u16*)d_in[8],(const u16*)d_in[9],(const u16*)d_in[10],(const u16*)d_in[11],(const u16*)d_in[12],(const u16*)d_in[13],Kk);
  conv_naive<<<512,256,0,stream>>>(Kk,u,(const u16*)d_in[14],out);
}
```

```cpp
#include <hip/hip_runtime.h>
#include <hip/hip_bf16.h>
#include <stdint.h>
#include <type_traits>

typedef unsigned short u16;
typedef short bf16x8 __attribute__((ext_vector_type(8)));
typedef short s16x4 __attribute__((ext_vector_type(4)));
typedef float f32x16 __attribute__((ext_vector_type(16)));
typedef float f32x4 __attribute__((ext_vector_type(4)));
typedef float f32x2 __attribute__((ext_vector_type(2)));
typedef __bf16 bf16x2_t __attribute__((ext_vector_type(2)));
typedef unsigned u32x4 __attribute__((ext_vector_type(4)));
typedef unsigned u32x2 __attribute__((ext_vector_type(2)));
typedef __attribute__((address_space(3))) char* lds_ptr;
typedef __attribute__((address_space(3))) const char* lds_cptr;

#define DI __device__ __forceinline__
#define MFMA(a, b, c) __builtin_amdgcn_mfma_f32_32x32x16_bf16((a), (b), (c), 0, 0, 0)
#define SBAR() __builtin_amdgcn_sched_barrier(0)
#define PIN(x) asm volatile("" : "+v"(x))
#define WAIT_BAR(N) asm volatile("s_waitcnt vmcnt(" #N ") lgkmcnt(0)\n\ts_barrier" ::: "memory")
#define LBAR() asm volatile("s_waitcnt lgkmcnt(0)\n\ts_barrier" ::: "memory")

DI float bf2f(u16 x) { return __uint_as_float(((unsigned)x) << 16); }
DI unsigned cvtpk(float lo, float hi) {
  f32x2 v = {lo, hi};
  bf16x2_t b = __builtin_convertvector(v, bf16x2_t);
  return __builtin_bit_cast(unsigned, b);
}
DI float bfround(float x) { return __uint_as_float(cvtpk(x, 0.f) << 16); }
DI unsigned rfl(unsigned x) { return __builtin_amdgcn_readfirstlane(x); }
DI bf16x8 lds128(lds_cptr p) { return *(const __attribute__((address_space(3))) bf16x8*)p; }
DI s16x4 ldstr(lds_cptr p) {
  return __builtin_amdgcn_ds_read_tr16_b64_v4i16((__attribute__((address_space(3))) s16x4*)p);
}
DI bf16x8 cat8(s16x4 a, s16x4 b) { return __builtin_shufflevector(a, b, 0, 1, 2, 3, 4, 5, 6, 7); }
template <int S> DI u32x4 pack8(const f32x16& x, float sc) {
  u32x4 w;
  w[0] = cvtpk(x[8 * S + 0] * sc, x[8 * S + 1] * sc);
  w[1] = cvtpk(x[8 * S + 2] * sc, x[8 * S + 3] * sc);
  w[2] = cvtpk(x[8 * S + 4] * sc, x[8 * S + 5] * sc);
  w[3] = cvtpk(x[8 * S + 6] * sc, x[8 * S + 7] * sc);
  return w;
}
template <int S> DI u32x4 pack8(const f32x16& x) {
  u32x4 w;
  w[0] = cvtpk(x[8 * S + 0], x[8 * S + 1]);
  w[1] = cvtpk(x[8 * S + 2], x[8 * S + 3]);
  w[2] = cvtpk(x[8 * S + 4], x[8 * S + 5]);
  w[3] = cvtpk(x[8 * S + 6], x[8 * S + 7]);
  return w;
}

constexpr float QSCALE = 0.08838834764831845f * 1.4426950408889634f;

constexpr int LDS_ALL = 5 * 32768;
constexpr int PW_OFF = 3 * 32768;

template <int OFF> DI void glds1(unsigned voff, const void* sbase, unsigned m0v) {
  asm volatile("s_mov_b32 m0, %2\n\ts_nop 0\n\tglobal_load_lds_dwordx4 %0, %1 offset:%3"
               :: "v"(voff), "s"(sbase), "s"(m0v), "i"(OFF) : "memory");
}
template <int N> DI void vm_bar() { asm volatile("s_waitcnt vmcnt(%0)\n\ts_barrier" :: "i"(N) : "memory"); }

struct ProjC {
  lds_cptr a_rd[4], a_rd2[4], w_rd[2];
  unsigned a_vo[2], w_vo, lds0, wu, rot;
  const char* a_src[2];
  const char* w_src[2];
  bf16x8 a[2][2], f0[2][2], f1[2][2];
};
template <int T, int P> DI void dmaA(const ProjC& c) {
  glds1<0>(c.a_vo[P & 1], c.a_src[T >> 3] + (((T + c.rot) & 7) * 128 + P * 8192), c.lds0 + (T % 3) * 32768 + c.wu * 4096 + P * 1024);
}
template <int U, int P> DI void dmaW(const ProjC& c) {
  const unsigned k0 = U < 16 ? ((U + 2 * c.rot) & 15) * 32 : ((U + c.rot) & 7) * 64;
  glds1<0>(c.w_vo, c.w_src[U >> 4] + (k0 + 4 * P) * 8192, c.lds0 + PW_OFF + (U & 3) * 16384 + c.wu * 2048 + P * 1024);
}
template <int T, int KS, int RT> DI bf16x8 rdA(const ProjC& c) {
  if constexpr (T % 3 == 2) return lds128(c.a_rd2[KS] + RT * 4096);
  else return lds128(c.a_rd[KS] + (T % 3) * 32768 + RT * 4096);
}
template <int KAP, int MAT, int TT, int H> DI s16x4 rdW(const ProjC& c) {
  if constexpr (KAP < 32) return ldstr(c.w_rd[TT] + ((KAP >> 1) & 3) * 16384 + MAT * 8192 + (KAP & 1) * 4096 + H * 1024);
  else return ldstr(c.w_rd[TT] + ((KAP >> 2) & 3) * 16384 + (KAP & 3) * 4096 + H * 1024);
}
DI void set_lo(bf16x8& f, s16x4 v) { f[0] = v[0]; f[1] = v[1]; f[2] = v[2]; f[3] = v[3]; }
DI void set_hi(bf16x8& f, s16x4 v) { f[4] = v[0]; f[5] = v[1]; f[6] = v[2]; f[7] = v[3]; }
template <int KAP, int R> DI void rd_next(ProjC& c) {
  constexpr int PB = KAP & 1, T = KAP >> 2, KS = KAP & 3;
  if constexpr (KAP < 32) {
    if constexpr (R == 0) c.a[PB][0] = rdA<T, KS, 0>(c);
    if constexpr (R == 1) set_lo(c.f0[PB][0], rdW<KAP, 0, 0, 0>(c));
    if constexpr (R == 2) set_hi(c.f0[PB][0], rdW<KAP, 0, 0, 1>(c));
    if constexpr (R == 3) set_lo(c.f1[PB][0], rdW<KAP, 1, 0, 0>(c));
    if constexpr (R == 4) set_hi(c.f1[PB][0], rdW<KAP, 1, 0, 1>(c));
    if constexpr (R == 5) c.a[PB][1] = rdA<T, KS, 1>(c);
    if constexpr (R == 6) set_lo(c.f0[PB][1], rdW<KAP, 0, 1, 0>(c));
    if constexpr (R == 7) set_hi(c.f0[PB][1], rdW<KAP, 0, 1, 1>(c));
    if constexpr (R == 8) set_lo(c.f1[PB][1], rdW<KAP, 1, 1, 0>(c));
    if constexpr (R == 9) set_hi(c.f1[PB][1], rdW<KAP, 1, 1, 1>(c));
  } else if constexpr (KAP < 64) {
    if constexpr (R == 0) c.a[PB][0] = rdA<T, KS, 0>(c);
    if constexpr (R == 1) set_lo(c.f0[PB][0], rdW<KAP, 0, 0, 0>(c));
    if constexpr (R == 2) set_hi(c.f0[PB][0], rdW<KAP, 0, 0, 1>(c));
    if constexpr (R == 3) c.a[PB][1] = rdA<T, KS, 1>(c);
    if constexpr (R == 4) set_lo(c.f0[PB][1], rdW<KAP, 0, 1, 0>(c));
    if constexpr (R == 5) set_hi(c.f0[PB][1], rdW<KAP, 0, 1, 1>(c));
  }
}
template <int U, int D, int LASTU> DI void dma_slot(const ProjC& c) {
  if constexpr (U < 16) {
    constexpr int T = (U >> 1) + 2;
    if constexpr (D < 2) {
      if constexpr (U <= 11 || (LASTU > 15 && U <= 13)) dmaA<T, 2 * (U & 1) + D>(c);
      if constexpr (LASTU > 15 && U == 14) { dmaA<9, 2 * D>(c); dmaA<9, 2 * D + 1>(c); }
    } else if constexpr (U + 3 <= LASTU) dmaW<U + 3, D - 2>(c);
  } else {
    if constexpr (D < 4) { if constexpr (U - 6 <= 15) dmaA<U - 6, D>(c); }
    else if constexpr (U + 3 <= LASTU) dmaW<U + 3, D - 4>(c);
  }
}
template <int KAP, int LASTU> DI void kv_kstep(ProjC& c, f32x16 (&aK)[2][2], f32x16 (&aV)[2][2]) {
  constexpr int PB = KAP & 1, U = KAP >> 1, NX = (KAP + 1 < 32 || LASTU > 15) ? KAP + 1 : 64;
#define KVG(G, R0, R1, ACC, MF) \
  rd_next<NX, R0>(c); if constexpr (R1 >= 0) rd_next<NX, (R1 >= 0 ? R1 : 0)>(c); SBAR(); \
  ACC = MF; PIN(ACC); \
  if constexpr (G == 2) dma_slot<U, 2 * PB, LASTU>(c); if constexpr (G == 6) dma_slot<U, 2 * PB + 1, LASTU>(c); \
  SBAR();
  KVG(0, 0, 1, aK[0][0], MFMA(c.f0[PB][0], c.a[PB][0], aK[0][0]))
  KVG(1, 2, 3, aV[0][0], MFMA(c.a[PB][0], c.f1[PB][0], aV[0][0]))
  KVG(2, 4, -1, aK[0][1], MFMA(c.f0[PB][0], c.a[PB][1], aK[0][1]))
  KVG(3, 5, -1, aV[0][1], MFMA(c.a[PB][1], c.f1[PB][0], aV[0][1]))
  KVG(4, 6, -1, aK[1][0], MFMA(c.f0[PB][1], c.a[PB][0], aK[1][0]))
  KVG(5, 7, -1, aV[1][0], MFMA(c.a[PB][0], c.f1[PB][1], aV[1][0]))
  KVG(6, 8, -1, aK[1][1], MFMA(c.f0[PB][1], c.a[PB][1], aK[1][1]))
  KVG(7, 9, -1, aV[1][1], MFMA(c.a[PB][1], c.f1[PB][1], aV[1][1]))
#undef KVG
}
template <int KAP, int LASTU, class HOOK> DI void q_kstep(ProjC& c, f32x16 (&aQ)[2][2], HOOK&& hook) {
  constexpr int PB = KAP & 1, U = 16 + ((KAP - 32) >> 2), KS = KAP & 3, NX = KAP + 1;
#define QG(G, R0, R1, ACC, MF) \
  rd_next<NX, R0>(c); if constexpr (R1 >= 0) rd_next<NX, (R1 >= 0 ? R1 : 0)>(c); SBAR(); \
  ACC = MF; PIN(ACC); \
  if constexpr (KS < 2 && (G & 1) == 0) dma_slot<U, 2 * KS + (G >> 1), LASTU>(c); \
  if constexpr (KS == 2 && G == 1) dma_slot<U, 4, LASTU>(c); if constexpr (KS == 3 && G == 0) dma_slot<U, 5, LASTU>(c); \
  hook(std::integral_constant<int, (KAP - 32) * 4 + G>{}); \
  SBAR();
  QG(0, 0, 1, aQ[0][0], MFMA(c.f0[PB][0], c.a[PB][0], aQ[0][0]))
  QG(1, 2, 3, aQ[0][1], MFMA(c.f0[PB][0], c.a[PB][1], aQ[0][1]))
  QG(2, 4, -1, aQ[1][0], MFMA(c.f0[PB][1], c.a[PB][0], aQ[1][0]))
  QG(3, 5, -1, aQ[1][1], MFMA(c.f0[PB][1], c.a[PB][1], aQ[1][1]))
#undef QG
}
DI void proj_init(ProjC& c, char* smem, int lane, int w, const u16* cA, int crow0, const u16* xA, int xrow0,
                  const u16* wk, const u16* wv, const u16* wq, int h) {
  const int r = lane & 31, hh = lane >> 5, rg = w >> 1, cg = w & 1;
  const lds_cptr L = (lds_cptr)smem;
  c.lds0 = (unsigned)(uintptr_t)smem; c.wu = w;
  const int sw = (r >> 1) & 7;
#pragma unroll
  for (int ks = 0; ks < 4; ++ks) { c.a_rd[ks] = L + (64 * rg + r) * 128 + (((2 * ks + hh) ^ sw) * 16); c.a_rd2[ks] = c.a_rd[ks] + 65536; }
  const int q4 = (lane >> 2) & 3, p4 = lane & 3, g1 = (lane >> 4) & 1;
#pragma unroll
  for (int tt = 0; tt < 2; ++tt) c.w_rd[tt] = L + PW_OFF + (8 * hh + q4) * 256 + (((2 * cg + tt) ^ q4) * 64) + g1 * 32 + p4 * 8;
  c.a_vo[0] = (lane >> 3) * 1024 + (((lane & 7) ^ ((lane >> 4) & 7)) * 16);
  c.a_vo[1] = (lane >> 3) * 1024 + (((lane & 7) ^ ((4 + (lane >> 4)) & 7)) * 16);
  c.w_vo = (lane >> 4) * 8192 + ((((lane >> 2) & 3) ^ (lane >> 4)) * 64) + (lane & 3) * 16;
  c.a_src[0] = (const char*)(cA + (size_t)crow0 * 512);
  c.a_src[1] = (const char*)(xA + (size_t)xrow0 * 512);
  c.w_src[0] = (const char*)((w < 4 ? wk : wv) + (size_t)(8 * (w & 3)) * 4096 + h * 128);
  c.w_src[1] = (const char*)(wq + (size_t)(8 * w) * 4096 + h * 128);
}
template <int... Is> struct iseq {};
template <int N, int... Is> struct mkseq : mkseq<N - 1, N - 1, Is...> {};
template <int... Is> struct mkseq<0, Is...> { using type = iseq<Is...>; };

template <int U, int NW, class PRE> DI void kv_unit(ProjC& c, f32x16 (&aK)[2][2], f32x16 (&aV)[2][2], PRE&& pre) {
  kv_kstep<2 * U, 23>(c, aK, aV);
  kv_kstep<2 * U + 1, 23>(c, aK, aV);
  pre();
  if constexpr (NW >= 0) vm_bar<NW>();
}

constexpr int K2_VOFF = 3 * 32768;
constexpr float SM_THR = 60.f;
constexpr float P_BIAS = -100.f, V_UP = 1125899906842624.f  , V_DN = 1.f / 1125899906842624.f;

DI float xhalf_max(float m) {
  auto rr = __builtin_amdgcn_permlane32_swap(__float_as_uint(m), __float_as_uint(m), false, false);
  return __builtin_fmaxf(__uint_as_float(rr[0]), __uint_as_float(rr[1]));
}
DI float xhalf_sum(float m) {
  auto rr = __builtin_amdgcn_permlane32_swap(__float_as_uint(m), __float_as_uint(m), false, false);
  return __uint_as_float(rr[0]) + __uint_as_float(rr[1]);
}
#define MX3(a, b, c) __builtin_fmaxf(__builtin_fmaxf((a), (b)), (c))
#define EX2(x) __builtin_amdgcn_exp2f(x)
struct DmaPlan { const char* vsrc; const char* ksrc; unsigned vm0, km0; unsigned twoV, doK, twoK; int mbase; lds_cptr vprev, knext; };
struct AttnC {
  f32x16 o[4];
  f32x16 negm;
  bf16x8 q[8];
  bf16x8 kf[16];
  bf16x8 vf[16];
  u32x4 p[4];
  f32x4 l;
  bf16x8 sel;
  float fpend;
  unsigned voff;
  bool pend;
};
constexpr int KPRE = 3;
DI bf16x8 rdK(lds_cptr kb_, int i) { return lds128(kb_ + (i & 1) * 8192 + (i >> 1) * 1024); }
DI bf16x8 rdV(lds_cptr vb_, int i) { return lds128(vb_ + (i >> 3) * 8192 + ((i & 3) * 2 + ((i >> 2) & 1)) * 1024); }
template <int E> DI void t_exp(f32x16 (&X)[2]) { float v = X[E >> 4][E & 15]; asm volatile("v_exp_f32 %0, %0" : "+v"(v)); X[E >> 4][E & 15] = v; }
template <int J> DI void t_cvt(AttnC& c, f32x16 (&X)[2]) {
  unsigned v;
  asm volatile("v_cvt_pk_bf16_f32 %0, %1, %2" : "=v"(v) : "v"(X[J >> 3][(2 * J) & 15]), "v"(X[J >> 3][(2 * J + 1) & 15]));
  c.p[J >> 2][J & 3] = v;
}
template <int OFF> DI void glds1c(unsigned voff, const void* sbase, unsigned m0v) {
  asm volatile("s_mov_b32 m0, %2\n\ts_nop 0\n\tglobal_load_lds_dwordx4 %0, %1 offset:%3 sc1"
               :: "v"(voff), "s"(sbase), "s"(m0v), "i"(OFF) : "memory");
}
DI unsigned sgnmask(int v) { unsigned m; asm("s_ashr_i32 %0, %1, 31" : "=s"(m) : "s"(v) : "scc"); return m; }
template <int OFF> DI void glds1p(unsigned voff, const void* sbase, unsigned m0v, unsigned pred) {
  asm volatile("s_mov_b32 m0, %2\n\ts_mov_b32 exec_lo, %4\n\ts_mov_b32 exec_hi, %4\n\tglobal_load_lds_dwordx4 %0, %1 offset:%3 sc1\n\ts_mov_b64 exec, -1"
               :: "v"(voff), "s"(sbase), "s"(m0v), "i"(OFF), "s"(pred) : "memory");
}
template <int I, bool HASPREV> DI void gapA(AttnC& c, f32x16 (&C)[2], f32x16 (&P)[2], lds_cptr kcur, lds_cptr vprev, const DmaPlan& dp) {
  if constexpr (I + KPRE < 16) c.kf[I + KPRE] = rdK(kcur, I + KPRE);
  else if constexpr (HASPREV) c.vf[I + KPRE - 16] = rdV(vprev, I + KPRE - 16);
  SBAR();
  if constexpr (I < 2) asm volatile("s_nop 1\n\tv_mfma_f32_32x32x16_bf16 %0, %1, %2, %3" : "=&v"(C[I & 1]) : "v"(c.kf[I]), "v"(c.q[0]), "v"(c.negm));
  else { C[I & 1] = MFMA(c.kf[I], c.q[I >> 1], C[I & 1]); PIN(C[I & 1]); }
  if constexpr (I == 1) glds1c<0>(c.voff, dp.vsrc, dp.vm0);
  if constexpr (I == 3) glds1c<1024>(c.voff, dp.vsrc, dp.vm0);
  if constexpr (I == 5) glds1p<2048>(c.voff, dp.vsrc, dp.vm0, dp.twoV);
  if constexpr (I == 7) glds1p<3072>(c.voff, dp.vsrc, dp.vm0, dp.twoV);
  if constexpr (I == 9) glds1p<0>(c.voff, dp.ksrc, dp.km0, dp.doK);
  if constexpr (I == 11) glds1p<1024>(c.voff, dp.ksrc, dp.km0, dp.doK);
  if constexpr (I == 13) glds1p<2048>(c.voff, dp.ksrc, dp.km0, dp.twoK);
  if constexpr (I == 15) glds1p<3072>(c.voff, dp.ksrc, dp.km0, dp.twoK);
  if constexpr (HASPREV) {
    t_exp<16 + I>(P);
    if constexpr ((I & 1) == 0) t_cvt<6 + (I >> 1)>(c, P);
  }
  SBAR();
}
template <int I, bool HASPREV> DI void gapB_m(AttnC& c, lds_cptr vprev, lds_cptr knext) {
  if constexpr (I + KPRE < 16) { if constexpr (HASPREV) c.vf[I + KPRE] = rdV(vprev, I + KPRE); }
  else c.kf[I + KPRE - 16] = rdK(knext, I + KPRE - 16);
  SBAR();
  if constexpr (HASPREV) { c.o[I & 3] = MFMA(c.vf[I], __builtin_bit_cast(bf16x8, c.p[I >> 2]), c.o[I & 3]); PIN(c.o[I & 3]); }
}
template <int K, bool HASPREV> DI void lsum(AttnC& c) {
  if constexpr (HASPREV) {
    c.l = __builtin_amdgcn_mfma_f32_16x16x32_bf16(c.sel, __builtin_bit_cast(bf16x8, c.p[K]), c.l, 0, 0, 0); PIN(c.l);
    SBAR();
  }
}
template <int I> DI void mask1(f32x16 (&C)[2], int thr, float ninf) {
  constexpr int key = (I & 3) + 8 * ((I >> 2) & 3) + 32 * (I >> 4);
  float v = C[I >> 4][I & 15];
  asm volatile("v_cmp_gt_i32 vcc, %3, %1\n\tv_cndmask_b32 %0, %0, %2, vcc" : "+v"(v) : "v"(thr), "v"(ninf), "i"(key) : "vcc");
  C[I >> 4][I & 15] = v;
}
template <int... Is> DI void mask_all(f32x16 (&C)[2], int thr, float ninf, iseq<Is...>) { (mask1<Is>(C, thr, ninf), ...); }
template <bool HASPREV, int... Is>
DI void phaseA(AttnC& c, f32x16 (&C)[2], f32x16 (&P)[2], lds_cptr kcur, lds_cptr vprev, const DmaPlan& dp, iseq<Is...>) {
  (gapA<Is, HASPREV>(c, C, P, kcur, vprev, dp), ...);
}
template <bool HASPREV, class NEXT>
DI void attn_step(AttnC& c, f32x16 (&C)[2], f32x16 (&P)[2], lds_cptr kcur, lds_cptr vprev, lds_cptr knext,
                  int qrow, bool first, const DmaPlan& dp, NEXT&& next) {
  phaseA<HASPREV>(c, C, P, kcur, vprev, dp, mkseq<16>::type{});
  gapB_m<0, HASPREV>(c, vprev, knext);
  if constexpr (HASPREV) { t_cvt<14>(c, P); t_cvt<15>(c, P); }
  SBAR();
  if (__builtin_expect(dp.mbase >= 0, 0)) {
    asm volatile("s_nop 7\n\ts_nop 7" ::: "memory");
    mask_all(C, qrow - dp.mbase, -__builtin_inff(), mkseq<32>::type{});
  }
  SBAR();
  float ma, mb;
  gapB_m<1, HASPREV>(c, vprev, knext);
  ma = MX3(C[0][0], C[0][1], C[0][2]); mb = MX3(C[0][3], C[0][4], C[0][5]); ma = MX3(ma, C[0][6], C[0][7]); mb = MX3(mb, C[0][8], C[0][9]);
  PIN(ma); PIN(mb); SBAR();
  gapB_m<2, HASPREV>(c, vprev, knext);
  ma = MX3(ma, C[0][10], C[0][11]); mb = MX3(mb, C[0][12], C[0][13]); ma = MX3(ma, C[0][14], C[0][15]); mb = MX3(mb, C[1][0], C[1][1]);
  PIN(ma); PIN(mb); SBAR();
  gapB_m<3, HASPREV>(c, vprev, knext);
  ma = MX3(ma, C[1][2], C[1][3]); mb = MX3(mb, C[1][4], C[1][5]); ma = MX3(ma, C[1][6], C[1][7]); mb = MX3(mb, C[1][8], C[1][9]);
  PIN(ma); PIN(mb); SBAR();
  lsum<0, HASPREV>(c);
  gapB_m<4, HASPREV>(c, vprev, knext);
  ma = MX3(ma, C[1][10], C[1][11]); mb = MX3(mb, C[1][12], C[1][13]); PIN(ma); PIN(mb);
  mb = MX3(mb, C[1][14], C[1][15]); PIN(mb);
  c.pend = false;
  const unsigned long long grow = __ballot(ma > P_BIAS + SM_THR) | __ballot(mb > P_BIAS + SM_THR);
  if (__builtin_expect((grow != 0ull) | first, 0)) {
    const float rm = xhalf_max(__builtin_fmaxf(ma, mb)) - P_BIAS;
    const float dl = first ? rm : __builtin_fmaxf(rm, 0.f);
    c.fpend = EX2(-__builtin_fmaxf(dl, 0.f));
    c.pend = !first;
    const float nm = c.negm[0] - dl;
#pragma unroll
    for (int i = 0; i < 16; ++i) { c.negm[i] = nm; C[0][i] -= dl; C[1][i] -= dl; }
  }
  SBAR();
  gapB_m<5, HASPREV>(c, vprev, knext);  t_exp<0>(C); t_exp<1>(C); SBAR();
  gapB_m<6, HASPREV>(c, vprev, knext);  t_exp<2>(C); t_exp<3>(C); next(std::integral_constant<int, 0>{}); SBAR();
  gapB_m<7, HASPREV>(c, vprev, knext);  t_exp<4>(C); t_cvt<0>(c, C); SBAR();
  lsum<1, HASPREV>(c);
  gapB_m<8, HASPREV>(c, vprev, knext);  t_exp<5>(C); t_exp<6>(C); SBAR();
  gapB_m<9, HASPREV>(c, vprev, knext);  t_exp<7>(C); t_cvt<1>(c, C); next(std::integral_constant<int, 1>{}); SBAR();
  gapB_m<10, HASPREV>(c, vprev, knext); t_exp<8>(C); t_exp<9>(C); SBAR();
  gapB_m<11, HASPREV>(c, vprev, knext); t_exp<10>(C); t_cvt<2>(c, C); next(std::integral_constant<int, 2>{}); SBAR();
  lsum<2, HASPREV>(c);
  gapB_m<12, HASPREV>(c, vprev, knext); t_exp<11>(C); t_exp<12>(C); SBAR();
  gapB_m<13, HASPREV>(c, vprev, knext); t_exp<13>(C); t_cvt<3>(c, C); next(std::integral_constant<int, 3>{}); SBAR();
  gapB_m<14, HASPREV>(c, vprev, knext); t_exp<14>(C); t_cvt<4>(c, C); SBAR();
  gapB_m<15, HASPREV>(c, vprev, knext); t_exp<15>(C); t_cvt<5>(c, C); SBAR();
  lsum<3, HASPREV>(c);
  if (__builtin_expect(c.pend, 0)) {
#pragma unroll
    for (int t = 0; t < 4; ++t)
#pragma unroll
      for (int i = 0; i < 16; ++i) c.o[t][i] *= c.fpend;
#pragma unroll
    for (int i = 0; i < 4; ++i) c.l[i] *= c.fpend;
  }
}
DI void st16c(void* sbase, unsigned voff, u32x4 v) {
  asm volatile("global_store_dwordx4 %0, %1, %2 sc1\n\ts_nop 1" :: "v"(voff), "v"(v), "s"(sbase) : "memory");
}
template <int OFF> DI void st16co(void* sbase, unsigned voff, u32x4 v) {
  asm volatile("global_store_dwordx4 %0, %1, %2 offset:%3 sc1\n\ts_nop 1" :: "v"(voff), "v"(v), "s"(sbase), "i"(OFF) : "memory");
}
DI u32x4 tr4x4(u32x4 d) {
  auto a = __builtin_amdgcn_permlane32_swap(d[0], d[2], false, false);
  auto b = __builtin_amdgcn_permlane32_swap(d[1], d[3], false, false);
  auto c = __builtin_amdgcn_permlane16_swap(a[0], b[0], false, false);
  auto e = __builtin_amdgcn_permlane16_swap(a[1], b[1], false, false);
  return u32x4{c[0], c[1], e[0], e[1]};
}
constexpr int B_CPY = 4160;
constexpr int B_U = 96 * 80;
constexpr int B_OFF_U = 33280;
constexpr int B_CH = B_OFF_U + B_U;
constexpr int B_WO = B_CH + 16640;
constexpr int B_YOUT = 2 * B_CH;
constexpr int B_YP = 72;
constexpr int G_T = 65536, G_P = 147456, G_L = G_P + 4096, G_R = G_L + 5632;
static_assert(B_CH % 16 == 0, "layout");
#define LDSP(T, p) ((__attribute__((address_space(3))) T*)(p))
#define LDSCP(T, p) ((const __attribute__((address_space(3))) T*)(p))

template <class FILL>
DI u32x4 ssm_kgen(unsigned ldt, char* smem, int lane, int w, FILL&& fill) {
  const int tid = w * 64 + lane;
  const int r = lane & 31, hh = lane >> 5;
  const int ch = w >> 2, v = w & 3;
  const int ct = tid & 255;
  const lds_ptr C0 = (lds_ptr)smem;
  if (tid < 256) {
    const lds_cptr rp = (lds_cptr)C0 + G_R + tid * 2;
    const float dt = bfround(expf(bf2f((u16)ldt)));
    const float ar = dt * bf2f(*LDSCP(u16, rp)), ai = dt * bf2f(*LDSCP(u16, rp + 512));
    const float br = dt * bf2f(*LDSCP(u16, rp + 1024)), bi = dt * bf2f(*LDSCP(u16, rp + 1536));
    const float cr = bf2f(*LDSCP(u16, rp + 2048)), ci = bf2f(*LDSCP(u16, rp + 2560));
    f32x4 pr = {ar * 1.4426950408889634f, ai * 0.15915494309189535f, cr * br - ci * bi, cr * bi + ci * br};
    *LDSP(f32x4, C0 + G_P + tid * 16) = pr;
  }
  LBAR();
  {
    int lai;
    if (r < 8) lai = r;
    else if (r < 12) lai = 256 + 2 * (r - 8);
    else if (r < 16) lai = 512 + 256 * ((r - 12) >> 1) + 4 * ((r - 12) & 1);
    else if (r < 21) lai = 1024 + 256 * (r - 16);
    else lai = 0;
    const float la = (float)lai, lb = (float)(8 * r);
    f32x16 acc = f32x16{};
    const lds_cptr pp = (lds_cptr)C0 + G_P + (ch * 128 + 32 * v + 4 * hh) * 16;
    f32x4 pa[16];
#pragma unroll
    for (int i = 0; i < 16; ++i) pa[i] = *LDSCP(f32x4, pp + (8 * (i >> 2) + (i & 3)) * 16);
#pragma unroll
    for (int ks = 0; ks < 4; ++ks) {
      u32x4 xh, xl, yh, yl;
#pragma unroll
      for (int m = 0; m < 4; ++m) {
        const f32x4 p = pa[4 * ks + m];
        const float e1 = __builtin_amdgcn_exp2f(p[0] * la), e2 = __builtin_amdgcn_exp2f(p[0] * lb);
        const float p1 = __builtin_amdgcn_fractf(p[1] * la), p2 = __builtin_amdgcn_fractf(p[1] * lb);
        const float er = e1 * __builtin_amdgcn_cosf(p1), ei = e1 * __builtin_amdgcn_sinf(p1);
        const float yr = e2 * __builtin_amdgcn_cosf(p2), yi = e2 * __builtin_amdgcn_sinf(p2);
        const float xr = p[2] * er - p[3] * ei, xn = -(p[2] * ei + p[3] * er);
        xh[m] = cvtpk(xr, xn);
        xl[m] = cvtpk(xr - __uint_as_float(xh[m] << 16), xn - __uint_as_float(xh[m] & 0xffff0000u));
        yh[m] = cvtpk(yr, yi);
        yl[m] = cvtpk(yr - __uint_as_float(yh[m] << 16), yi - __uint_as_float(yh[m] & 0xffff0000u));
      }
      acc = MFMA(__builtin_bit_cast(bf16x8, xl), __builtin_bit_cast(bf16x8, yh), acc);
      acc = MFMA(__builtin_bit_cast(bf16x8, xh), __builtin_bit_cast(bf16x8, yl), acc);
      acc = MFMA(__builtin_bit_cast(bf16x8, xh), __builtin_bit_cast(bf16x8, yh), acc);
      PIN(acc);
      fill(ks);
      SBAR();
    }
#pragma unroll
    for (int i = 0; i < 12; ++i) {
      const int row = (i & 3) + 8 * (i >> 2) + 4 * hh;
      *LDSP(float, C0 + G_T + w * 3072 + (row * 32 + r) * 4) = acc[i];
    }
  }
  LBAR();
  if (ct < 168) {
    const int row = ct >> 3, j0 = (ct & 7) * 4;
    const lds_cptr tp = (lds_cptr)C0 + G_T + ch * 12288 + (row * 32 + j0) * 4;
    const f32x4 t = (*LDSCP(f32x4, tp) + *LDSCP(f32x4, tp + 3072)) + (*LDSCP(f32x4, tp + 6144) + *LDSCP(f32x4, tp + 9216));
    int base, step;
    if (row < 8) { base = row; step = 8; }
    else if (row < 12) { base = 256 + (row - 8); step = 4; }
    else if (row < 16) { base = 384 + 64 * ((row - 12) >> 1) + ((row - 12) & 1); step = 2; }
    else { base = 512 + 32 * (row - 16); step = 1; }
#pragma unroll
    for (int e = 0; e < 4; ++e) *LDSP(float, C0 + G_L + ch * 2816 + (base + (j0 + e) * step) * 4) = t[e];
  }
  LBAR();
  float kv[8];
#pragma unroll
  for (int i = 0; i < 8; ++i) {
    const int l = 8 * ct + i;
    const int rk = l < 128 ? l : (int)(cvtpk((float)l, 0.f) & 0xffffu) - (0x4300 - 128);
    kv[i] = *LDSCP(float, (lds_cptr)C0 + G_L + ch * 2816 + rk * 4);
  }
  return u32x4{cvtpk(kv[0], kv[1]), cvtpk(kv[2], kv[3]), cvtpk(kv[4], kv[5]), cvtpk(kv[6], kv[7])};
}

struct SsmIn { u32x4 uu, ka, kb; unsigned dsk; };

template <class FILL>
DI void ssm_conv(const SsmIn& in, char* smem, int lane, int w, FILL&& fill) {
  const int tid = w * 64 + lane;
  const int r = lane & 31, hh = lane >> 5;
  const int ch = w >> 2, v = w & 3;
  const int ct = tid & 255;
  const lds_ptr C0 = (lds_ptr)smem;
  const lds_ptr CH = C0 + ch * B_CH;
  if (ct < 160) *LDSP(u32x4, CH + B_OFF_U + ct * 16) = u32x4{0, 0, 0, 0};
  {
    const int off = B_OFF_U + (32 + (tid >> 3)) * 80 + (tid & 7) * 8;
    *LDSP(u32x2, C0 + off) = u32x2{__builtin_amdgcn_perm(in.uu[1], in.uu[0], 0x05040100u), __builtin_amdgcn_perm(in.uu[3], in.uu[2], 0x05040100u)};
    *LDSP(u32x2, C0 + B_CH + off) = u32x2{__builtin_amdgcn_perm(in.uu[1], in.uu[0], 0x07060302u), __builtin_amdgcn_perm(in.uu[3], in.uu[2], 0x07060302u)};
  }
  {
    const u32x4 a = ct ? in.ka : u32x4{0, 0, 0, 0}, b = in.kb;
    const u32x4 c1 = {__builtin_amdgcn_alignbyte(b[0], a[3], 2), __builtin_amdgcn_alignbyte(b[1], b[0], 2),
                      __builtin_amdgcn_alignbyte(b[2], b[1], 2), __builtin_amdgcn_alignbyte(b[3], b[2], 2)};
    *LDSP(u32x4, CH + 0 * B_CPY + 64 + ct * 16) = b;
    *LDSP(u32x4, CH + 1 * B_CPY + 64 + ct * 16) = c1;
    *LDSP(u32x4, CH + 2 * B_CPY + 64 + ct * 16) = u32x4{a[3], b[0], b[1], b[2]};
    *LDSP(u32x4, CH + 3 * B_CPY + 64 + ct * 16) = u32x4{__builtin_amdgcn_alignbyte(a[3], a[2], 2), c1[0], c1[1], c1[2]};
    if (ct < 16) *LDSP(u32x4, CH + (ct >> 2) * B_CPY + (ct & 3) * 16) = u32x4{0, 0, 0, 0};
  }
  fill(0); fill(1);
  LBAR();
  f32x16 y0 = f32x16{}, y1 = f32x16{};
  {
    const int q4 = (lane >> 2) & 3, p4 = lane & 3, g1 = (lane >> 4) & 1;
    const lds_cptr fbase = (lds_cptr)CH + q4 * B_CPY + (32 - 8 * hh + 16 * g1 + 4 * p4) * 2 + 512 * v - 40;
    const lds_cptr gbase = (lds_cptr)CH + B_OFF_U + (32 + r - 8 * v - 7) * 80 + hh * 16;
#pragma unroll
    for (int dd = 0; dd < 8; ++dd) {
#pragma unroll
      for (int e = 0; e < 2; ++e) {
        const int o = 64 * dd + 40 - 32 * e;
        const bf16x8 flo = cat8(ldstr(fbase + o), ldstr(fbase + o - 8));
        const bf16x8 fhi = cat8(ldstr(fbase + o + 2048), ldstr(fbase + o + 2048 - 8));
        const bf16x8 gneg = lds128(gbase + (7 - dd) * 80 + e * 32);
        const bf16x8 gpos = lds128(gbase + (39 - dd) * 80 + e * 32);
        y0 = MFMA(flo, gneg, y0);
        y1 = MFMA(flo, gpos, y1);
        y1 = MFMA(fhi, gneg, y1);
        SBAR(); fill(2 + 2 * dd + e); SBAR();
      }
    }
  }
  LBAR();
#pragma unroll
  for (int qd = 0; qd < 4; ++qd) {
    *LDSP(f32x4, CH + v * 8192 + qd * 1024 + lane * 16) = f32x4{y0[4 * qd], y0[4 * qd + 1], y0[4 * qd + 2], y0[4 * qd + 3]};
    *LDSP(f32x4, CH + v * 8192 + 4096 + qd * 1024 + lane * 16) = f32x4{y1[4 * qd], y1[4 * qd + 1], y1[4 * qd + 2], y1[4 * qd + 3]};
  }
  fill(18); fill(19);
  LBAR();
  fill(20);
  {
    const float dsk = __uint_as_float(ch ? (in.dsk & 0xffff0000u) : (in.dsk << 16));
    const int arow = 32 * (v >> 1) + r;
#pragma unroll
    for (int k = 0; k < 2; ++k) {
      const int qd = 2 * (v & 1) + k;
      const lds_cptr pp = (lds_cptr)CH + (v >> 1) * 4096 + qd * 1024 + lane * 16;
      const f32x4 s0 = *LDSCP(f32x4, pp), s1 = *LDSCP(f32x4, pp + 8192), s2 = *LDSCP(f32x4, pp + 16384), s3 = *LDSCP(f32x4, pp + 24576);
      const int b0 = 8 * qd + 4 * hh;
      const u32x2 uw = *LDSCP(u32x2, CH + B_OFF_U + (32 + arow) * 80 + b0 * 2);
      float res[4];
#pragma unroll
      for (int e = 0; e < 4; ++e) {
        const float ys = (s0[e] + s1[e]) + (s2[e] + s3[e]);
        const float uv = __uint_as_float((e & 1) ? (uw[e >> 1] & 0xffff0000u) : (uw[e >> 1] << 16));
        res[e] = bfround(ys) + bfround(uv * dsk);
      }
      *LDSP(u32x2, C0 + B_YOUT + (ch * 64 + arow) * B_YP + b0 * 2) = u32x2{cvtpk(res[0], res[1]), cvtpk(res[2], res[3])};
    }
  }
  LBAR();
}
constexpr int PD = 8;
struct OpRing { bf16x8 f[PD][4]; unsigned rot; };
template <int K> DI void op_load(OpRing& R, unsigned voff, const char* a, const char* b) {
  const unsigned ko = (((K >> 2) + R.rot) & 7) * 4096;
  const char* a0 = a + ko; const char* b0 = b + ko;
  asm volatile("global_load_dwordx4 %0, %4, %5 offset:%9 sc1\n\tglobal_load_dwordx4 %1, %4, %6 offset:%9 sc1\n\t"
               "global_load_dwordx4 %2, %4, %7 offset:%9 sc1\n\tglobal_load_dwordx4 %3, %4, %8 offset:%9 sc1"
               : "=&v"(R.f[K % PD][0]), "=&v"(R.f[K % PD][1]), "=&v"(R.f[K % PD][2]), "=&v"(R.f[K % PD][3])
               : "v"(voff), "s"(a0), "s"(b0), "s"(a0 + 262144), "s"(b0 + 262144), "i"((K & 3) * 1024) : "memory");
}
template <int K, int NK> DI void op_step(OpRing& R, f32x16 (&acc)[2][2], unsigned voff, const char* a, const char* b) {
  constexpr int rem = (NK - 1 - K) < (PD - 1) ? (NK - 1 - K) : (PD - 1);
  asm volatile("s_waitcnt vmcnt(%4)" : "+v"(R.f[K % PD][0]), "+v"(R.f[K % PD][1]), "+v"(R.f[K % PD][2]), "+v"(R.f[K % PD][3]) : "i"(4 * rem) : "memory");
  acc[0][0] = MFMA(R.f[K % PD][0], R.f[K % PD][1], acc[0][0]);
  acc[1][0] = MFMA(R.f[K % PD][2], R.f[K % PD][1], acc[1][0]);
  acc[0][1] = MFMA(R.f[K % PD][0], R.f[K % PD][3], acc[0][1]);
  acc[1][1] = MFMA(R.f[K % PD][2], R.f[K % PD][3], acc[1][1]);
  PIN(acc[0][0]); PIN(acc[1][0]); PIN(acc[0][1]); PIN(acc[1][1]);
  SBAR();
  if constexpr (K + PD < NK) op_load<K + PD>(R, voff, a, b);
  SBAR();
}
template <int... Ks> DI void op_pro(OpRing& R, unsigned voff, const char* a, const char* b, iseq<Ks...>) { (op_load<Ks>(R, voff, a, b), ...); }
template <int... Ks> DI void op_steps(OpRing& R, f32x16 (&acc)[2][2], unsigned voff, const char* a, const char* b, iseq<Ks...>) {
  (op_step<Ks, sizeof...(Ks)>(R, acc, voff, a, b), ...);
}
DI void outproj8(const char* ctxf, const char* Wof, u16* __restrict__ out, char* smem, int qb64, int mb64, int lane, int w) {
  const int r = lane & 31, hh = lane >> 5;
  f32x16 acc[2][2];
#pragma unroll
  for (int i = 0; i < 2; ++i)
#pragma unroll
    for (int j = 0; j < 2; ++j) acc[i][j] = f32x16{};
  {
    OpRing R;
    R.rot = (mb64 + 2 * (qb64 & 1) + 4 * (qb64 >> 4)) & 7;
    const char* a = Wof + ((size_t)(mb64 * 2) * 256 + w * 32) * 1024;
    const char* b = ctxf + ((size_t)(qb64 * 2) * 256 + w * 32) * 1024;
    op_pro(R, lane * 16, a, b, mkseq<PD>::type{});
    op_steps(R, acc, lane * 16, a, b, mkseq<32>::type{});
  }
  const lds_ptr Lw = (lds_ptr)smem + lane * 16;
#pragma unroll
  for (int t = 0; t < 4; ++t)
#pragma unroll
    for (int qd = 0; qd < 4; ++qd)
      *LDSP(f32x4, Lw + w * 16384 + (t * 4 + qd) * 1024) =
          f32x4{acc[t >> 1][t & 1][4 * qd], acc[t >> 1][t & 1][4 * qd + 1], acc[t >> 1][t & 1][4 * qd + 2], acc[t >> 1][t & 1][4 * qd + 3]};
  LBAR();
  const int t = w >> 1, mi = t >> 1, qi = t & 1;
  u16* dst = out + (size_t)(qb64 * 64 + qi * 32 + r) * 1024 + mb64 * 64 + mi * 32 + 4 * hh + 16 * (w & 1);
#pragma unroll
  for (int k = 0; k < 2; ++k) {
    const lds_cptr pp = (lds_cptr)Lw + (w * 2 + k) * 1024;
    f32x4 s[8];
#pragma unroll
    for (int j = 0; j < 8; ++j) s[j] = *LDSCP(f32x4, pp + j * 16384);
    const f32x4 o = ((s[0] + s[1]) + (s[2] + s[3])) + ((s[4] + s[5]) + (s[6] + s[7]));
    *(u32x2*)(dst + 8 * k) = u32x2{cvtpk(o[0], o[1]), cvtpk(o[2], o[3])};
  }
}

typedef __attribute__((address_space(1))) unsigned gu32;
constexpr unsigned TAGC = 0x9E3779B9u;
constexpr int SY_KV = 256, SY_CTX = 4096, SY_U = 8192, SY_M = 12288;
constexpr unsigned SPIN_CAP = 1u << 18;
DI void wait_flags(gu32* f, int n, unsigned tag, int lane) {
  for (unsigned sp = 0; sp < SPIN_CAP; ++sp) {
    unsigned v = tag;
    if (lane < n) v = __hip_atomic_load(f + lane, __ATOMIC_RELAXED, __HIP_MEMORY_SCOPE_AGENT);
    if (__all(v == tag)) break;
    __builtin_amdgcn_s_sleep(2);
  }
  asm volatile("" ::: "memory");
}
template <int U, class HOOK> DI void q_unit(ProjC& c, f32x16 (&aQ)[2][2], HOOK&& hook) {
  q_kstep<32 + 4 * (U - 16), 23>(c, aQ, hook); q_kstep<33 + 4 * (U - 16), 23>(c, aQ, hook);
  q_kstep<34 + 4 * (U - 16), 23>(c, aQ, hook); q_kstep<35 + 4 * (U - 16), 23>(c, aQ, hook);
}
struct Params {
  const u16 *x, *c, *wq, *wk, *wv, *wo, *u, *Are, *Aim, *Bre, *Bim, *Cre, *Cim, *logdt, *Dskip;
  u16* out; char* ws;
};

__global__ __launch_bounds__(512, 2) void mega(Params P) {
  extern __shared__ __attribute__((aligned(16))) char smem[];
  const int tid = threadIdx.x, lane = tid & 63, w = rfl(tid >> 6);
  const int r = lane & 31, hh = lane >> 5;
  const int bid = blockIdx.x, xcd = bid & 7, idx = bid >> 3;
  const int h = xcd * 4 + (idx & 3), x = idx >> 2;
  const int g = w >> 2;
  const int lo = x, hi = 15 - x;
  const int n1 = 2 * x + 2;
  const unsigned lds0 = (unsigned)(uintptr_t)smem;
  const unsigned voff = lane * 16;
  gu32* sync = (gu32*)P.ws;
  char* Kf = P.ws + ((size_t)16 << 20);
  char* Vf = P.ws + ((size_t)32 << 20);
  char* ctxf = P.ws + ((size_t)48 << 20);
  char* Wof = P.ws + ((size_t)64 << 20);
  char* uT = P.ws + ((size_t)68 << 20);
  char* yT = P.ws + ((size_t)70 << 20);
  char* kT = P.ws + ((size_t)72 << 20) + (size_t)bid * 8448 + 128;
  const char* Kh = Kf + (size_t)h * 32 * 16384;
  const char* Vh = Vf + (size_t)h * 32 * 16384;
  AttnC c;
  c.voff = voff;
  const int rg_hi = g ? (7 - w) : w;
  const int qb_hi = hi * 4 + rg_hi;
  const int qb_lo = lo * 4 + w;
  unsigned tag;
  const int d0 = xcd * 64 + idx * 2;
  SsmIn si;
  {
    unsigned ldt;
    {
      const u16* parr = w == 0 ? P.Are : w == 1 ? P.Aim : w == 2 ? P.Bre : w == 3 ? P.Bim : w == 4 ? P.Cre : P.Cim;
      const unsigned pm = sgnmask(w - 6);
      asm volatile("s_mov_b32 m0, %2\n\ts_mov_b32 exec_lo, %3\n\ts_mov_b32 exec_hi, 0\n\tglobal_load_lds_dwordx4 %0, %1\n\ts_mov_b64 exec, -1"
                   :: "v"(voff), "s"(parr + d0 * 128), "s"(lds0 + G_R + w * 512), "s"(pm) : "memory");
      asm volatile("global_load_ushort %0, %1, %2\n\ts_barrier" : "=v"(ldt) : "v"((unsigned)(((tid >> 7) & 1) * 2)), "s"(P.logdt + d0) : "memory");
    }
    ProjC pc;
    proj_init(pc, smem, lane, w, P.c, x * 256 + 32 * w, P.x, (g ? hi : lo) * 128 + 32 * (w & 3), P.wk, P.wv, P.wq, h);
    pc.rot = (x + 2 * (idx & 3)) & 7;
    dmaA<0, 0>(pc); dmaA<0, 1>(pc); dmaA<0, 2>(pc); dmaA<0, 3>(pc);
    dmaW<0, 0>(pc); dmaW<0, 1>(pc);
    u32x4 utile;
    const unsigned tr_go = (4 * ((lane >> 3) & 1) + (lane >> 4)) * 1024 + (lane & 7) * 16;
    const unsigned tr_so = (4 * (lane & 7) + (lane >> 4)) * 8192 + ((lane >> 3) & 1) * 16;
    unsigned salt;
    asm volatile("s_waitcnt vmcnt(6)\n\ts_barrier" : "+v"(ldt) :: "memory");
    {
      auto fill0 = [&](int i) {
        if (i == 0) { dmaW<1, 0>(pc); dmaW<1, 1>(pc); }
        if (i == 1) { dmaA<1, 0>(pc); dmaA<1, 1>(pc); dmaA<1, 2>(pc); }
        if (i == 2) { dmaA<1, 3>(pc); dmaW<2, 0>(pc); dmaW<2, 1>(pc); }
        if (i == 3) {
          asm volatile("global_load_dwordx4 %0, %1, %2" : "=v"(utile) : "v"(tr_go), "s"((const char*)(P.u + (size_t)(64 * idx + 8 * w) * 512 + 64 * xcd)) : "memory");
          unsigned zero = 0;
          asm volatile("global_load_dword %0, %1, %2 sc1" : "=v"(salt) : "v"(zero), "s"(P.ws) : "memory");
        }
      };
      const u32x4 klin = ssm_kgen(ldt, smem, lane, w, fill0);
      asm volatile("global_store_dwordx4 %0, %1, %2\n\ts_nop 1" :: "v"((unsigned)(tid * 16)), "v"(klin), "s"(kT) : "memory");
    }
    f32x16 aK[2][2], aV[2][2];
#pragma unroll
    for (int i = 0; i < 2; ++i)
#pragma unroll
      for (int j = 0; j < 2; ++j) { aK[i][j] = f32x16{}; aV[i][j] = f32x16{}; }
    vm_bar<9>();
    rd_next<0, 0>(pc); rd_next<0, 1>(pc); rd_next<0, 2>(pc); rd_next<0, 3>(pc); rd_next<0, 4>(pc);
    rd_next<0, 5>(pc); rd_next<0, 6>(pc); rd_next<0, 7>(pc); rd_next<0, 8>(pc); rd_next<0, 9>(pc);
    auto nop = [&]() {};
    unsigned uflag;
    kv_unit<0, 7>(pc, aK, aV, nop);
    kv_unit<1, 4>(pc, aK, aV, nop);
    asm volatile("" : "+v"(salt), "+v"(utile));
    tag = rfl(salt) + TAGC;
    unsigned tch = 0;
    auto touch = [&](int j) {
      const char* p = j < 8 ? (const char*)(P.x + (size_t)(idx * 64 + 8 * j + w) * 512) : (const char*)(P.wq + (size_t)(idx * 16 + 8 * (j - 8) + w) * 4096 + xcd * 512);
      asm volatile("s_mov_b64 exec, 0xff\n\tglobal_load_dword %0, %1, %2\n\ts_mov_b64 exec, -1" : "+v"(tch) : "v"((unsigned)(lane * 128)), "s"(p) : "memory");
    };
    kv_unit<2, 5>(pc, aK, aV, [&]() { st16c(uT + ((size_t)xcd * 32 * 2048 + 64 * idx + 8 * w) * 4, tr_so, tr4x4(utile)); });
    kv_unit<3, 6>(pc, aK, aV, [&]() { touch(0); });
    kv_unit<4, 6>(pc, aK, aV, [&]() { touch(1); });
    if (w == 0 && lane == 0) __hip_atomic_store(sync + SY_U + xcd * 64 + idx, tag, __ATOMIC_RELAXED, __HIP_MEMORY_SCOPE_AGENT);
    kv_unit<5, 6>(pc, aK, aV, [&]() { touch(2); });
    kv_unit<6, 6>(pc, aK, aV, [&]() { touch(3); });
    kv_unit<7, 6>(pc, aK, aV, [&]() { touch(4); });
    kv_unit<8, 6>(pc, aK, aV, [&]() { touch(5); });
    kv_unit<9, 6>(pc, aK, aV, [&]() { touch(6); });
    kv_unit<10, 6>(pc, aK, aV, [&]() { touch(7); });
    kv_unit<11, 6>(pc, aK, aV, [&]() { touch(8); });
    kv_unit<12, 6>(pc, aK, aV, [&]() { touch(9); });
    kv_unit<13, 6>(pc, aK, aV, [&]() {
      asm volatile("global_load_dword %0, %1, %2 sc1" : "=v"(uflag) : "v"((unsigned)(r * 4)), "s"(sync + SY_U + xcd * 64) : "memory");
    });
    kv_unit<14, 7>(pc, aK, aV, nop);
    kv_unit<15, 2>(pc, aK, aV, nop);
    asm volatile("" : "+v"(uflag), "+v"(tch));
    if (__builtin_expect(!__all(uflag == tag), 0)) wait_flags(sync + SY_U + xcd * 64, 32, tag, lane);
    const int rg = w >> 1, cg = w & 1;
    char* kdst = Kf + ((size_t)(h * 64 + x * 8 + rg * 2) * 8 + 4 * cg) * 1024;
    char* vdst = Vf + ((size_t)(h * 64 + x * 8 + rg * 2) * 8 + 4 * cg) * 1024;
    auto kvstore = [&](int i) {
      const int mat = i >> 3, rt = (i >> 2) & 1, tt = (i >> 1) & 1;
      const f32x16& A = mat ? aV[tt][rt] : aK[tt][rt];
      if (mat) st16c(vdst + rt * 8192 + tt * 2048 + (i & 1) * 1024, voff, (i & 1) ? pack8<1>(A, V_UP) : pack8<0>(A, V_UP));
      else st16c(kdst + rt * 8192 + tt * 2048 + (i & 1) * 1024, voff, (i & 1) ? pack8<1>(A) : pack8<0>(A));
    };
    f32x16 aQ[2][2];
#pragma unroll
    for (int i = 0; i < 2; ++i)
#pragma unroll
      for (int j = 0; j < 2; ++j) aQ[i][j] = f32x16{};
    auto hook = [&](auto G) {
      constexpr int gi = decltype(G)::value, un = gi >> 4, gg = gi & 15;
      if constexpr (un < 6 && gg == 13) kvstore(un);
      if constexpr (un == 5 && gg == 14) {
        const unsigned z0 = 0;
        asm volatile("global_load_dwordx4 %0, %1, %2 sc1" : "=v"(si.uu) : "v"((unsigned)(tid * 16)), "s"(uT + (size_t)(xcd * 32 + idx) * 8192) : "memory");
        asm volatile("global_load_dwordx4 %0, %1, %2 offset:-16 sc1" : "=v"(si.ka) : "v"((unsigned)(tid * 16)), "s"(kT) : "memory");
        asm volatile("global_load_dwordx4 %0, %1, %2 sc1" : "=v"(si.kb) : "v"((unsigned)(tid * 16)), "s"(kT) : "memory");
        asm volatile("global_load_dword %0, %1, %2" : "=v"(si.dsk) : "v"(z0), "s"(P.Dskip + d0) : "memory");
      }
      if constexpr (un == 6 && gg == 4) kvstore(6);
      if constexpr (un == 6 && gg == 12) kvstore(7);
      if constexpr (un == 7 && (gg == 0 || gg == 2)) {
        constexpr int k = gg >> 1;
        glds1<0>((unsigned)((((lane >> 2) ^ ((2 * w + k) & 3)) * 64) + (lane & 3) * 16), (const char*)(P.wo + (size_t)(128 * h + 16 * x + 2 * w + k) * 512),
                 lds0 + B_WO + (2 * w + k) * 1024);
      }
      if constexpr (un == 7 && gg == 5) kvstore(8);
      if constexpr (un == 7 && gg == 13) kvstore(9);
    };
    q_unit<16>(pc, aQ, hook); vm_bar<3>();
    q_unit<17>(pc, aQ, hook); vm_bar<3>();
    q_unit<18>(pc, aQ, hook); vm_bar<3>();
    q_unit<19>(pc, aQ, hook); vm_bar<3>();
    q_unit<20>(pc, aQ, hook); vm_bar<3>();
    q_unit<21>(pc, aQ, hook); vm_bar<5>();
    q_unit<22>(pc, aQ, hook); vm_bar<7>();
    q_unit<23>(pc, aQ, hook);
    asm volatile("s_waitcnt vmcnt(6)\n\ts_barrier" : "+v"(si.uu), "+v"(si.ka), "+v"(si.kb), "+v"(si.dsk) :: "memory");
#pragma unroll
    for (int rt = 0; rt < 2; ++rt) {
      const int rho = 2 * rg + rt;
      const unsigned qb_ = K2_VOFF + ((rho >> 1) & 1) * 32768 + (rho >> 2) * 16384 + (rho & 1) * 8192;
#pragma unroll
      for (int tt = 0; tt < 2; ++tt) {
        const lds_ptr dst = (lds_ptr)smem + qb_ + (2 * (2 * cg + tt)) * 1024 + lane * 16;
        *(__attribute__((address_space(3))) u32x4*)(dst) = pack8<0>(aQ[tt][rt], QSCALE);
        *(__attribute__((address_space(3))) u32x4*)(dst + 1024) = pack8<1>(aQ[tt][rt], QSCALE);
      }
    }
    unsigned kvflag;
    {
      auto fill = [&](int i) {
        if (i == 0) kvstore(10);
        if (i == 1) asm volatile("s_waitcnt vmcnt(3)" ::: "memory");
        if (i == 3) kvstore(11);
        if (i == 5) kvstore(12);
        if (i == 7) kvstore(13);
        if (i == 9) kvstore(14);
        if (i == 11) kvstore(15);
        if (i == 13) {
          const int q4 = (lane >> 2) & 3, p4 = lane & 3, g1 = (lane >> 4) & 1;
#pragma unroll
          for (int k = 0; k < 2; ++k) {
            const int mb = 2 * w + k;
            const lds_cptr p = (lds_cptr)smem + B_WO + (4 * hh + q4) * 1024 + ((mb ^ q4) * 64) + g1 * 32 + p4 * 8;
            const s16x4 lo_ = ldstr(p), hi_ = ldstr(p + 8 * 1024);
            st16c(Wof + ((size_t)(mb * 256 + 8 * h + x) * 1024), voff, __builtin_bit_cast(u32x4, cat8(lo_, hi_)));
          }
        }
        if (i == 17) asm volatile("s_waitcnt vmcnt(2)" ::: "memory");
        if (i == 18) { if (w == 0 && lane == 0) __hip_atomic_store(sync + SY_KV + h * 64 + x, tag, __ATOMIC_RELAXED, __HIP_MEMORY_SCOPE_AGENT); }
        if (i == 20) asm volatile("global_load_dword %0, %1, %2 sc1" : "=v"(kvflag) : "v"((unsigned)((lane & 7) * 4)), "s"(P.ws + (SY_KV + h * 64) * 4) : "memory");
      };
      ssm_conv(si, smem, lane, w, fill);
    }
    {
      const lds_cptr yp = (lds_cptr)smem + B_YOUT + (tid >> 3) * B_YP + (tid & 7) * 8;
      const u32x2 y0 = *LDSCP(u32x2, yp), y1 = *LDSCP(u32x2, yp + 64 * B_YP);
      const u32x4 yo = {__builtin_amdgcn_perm(y1[0], y0[0], 0x05040100u), __builtin_amdgcn_perm(y1[0], y0[0], 0x07060302u),
                        __builtin_amdgcn_perm(y1[1], y0[1], 0x05040100u), __builtin_amdgcn_perm(y1[1], y0[1], 0x07060302u)};
      st16c(yT + (size_t)(xcd * 32 + idx) * 8192, (unsigned)(tid * 16), yo);
    }
    asm volatile("s_waitcnt vmcnt(1)" : "+v"(kvflag) :: "memory");
    if (__builtin_expect(!__all(kvflag == tag), 0)) wait_flags(sync + SY_KV + h * 64, 8, tag, lane);
    glds1c<0>(voff, Kh + w * 2048, lds0 + 32768 + w * 2048); glds1c<1024>(voff, Kh + w * 2048, lds0 + 32768 + w * 2048);
    glds1c<0>(voff, Kh + 16384 + w * 2048, lds0 + 65536 + w * 2048); glds1c<1024>(voff, Kh + 16384 + w * 2048, lds0 + 65536 + w * 2048);
  }
  const lds_cptr L = (lds_cptr)smem + lane * 16;
  const int rho_hi = 4 + rg_hi;
  const unsigned qoff_hi = K2_VOFF + ((rho_hi >> 1) & 1) * 32768 + 16384 + (rho_hi & 1) * 8192;
  const unsigned qoff_0 = g ? qoff_hi : (unsigned)(K2_VOFF + ((w >> 1) & 1) * 32768 + (w & 1) * 8192);
#pragma unroll
  for (int f = 0; f < 8; ++f) c.q[f] = lds128(L + qoff_0 + f * 1024);
  WAIT_BAR(0);
  if (w == 0 && lane == 0) __hip_atomic_store(sync + SY_M + bid, tag, __ATOMIC_RELAXED, __HIP_MEMORY_SCOPE_AGENT);
  const unsigned w2k = w * 2048, g16k = g * 16384;
  const int ms1 = g ? -100 : n1 - 2;
  unsigned tm1 = 0, t0 = 0, t1 = 0, t2 = sgnmask(n1 - 3);
  unsigned o0 = w2k, o1 = 16384 + w2k, o2 = 32768 + w2k + (t2 & w2k);
  auto plan_part = [&](auto PART, DmaPlan& dp, int s, unsigned vslot, unsigned kslot, unsigned knslot) {
    constexpr int part = decltype(PART)::value;
    if constexpr (part == 0) {
      const unsigned t3 = sgnmask(n1 - 3 - s);
      const unsigned o3 = o2 + 16384u + (t2 & 16384u) + ((t3 & ~t2) & w2k);
      tm1 = t0; t0 = t1; t1 = t2; t2 = t3; o0 = o1; o1 = o2; o2 = o3;
      asm volatile("" : "+s"(t2), "+s"(o2));
    } else if constexpr (part == 1) {
      dp.twoV = t0;
      dp.vsrc = Vh + o0;
      dp.vm0 = lds0 + vslot + w2k + (t0 & w2k);
      dp.ksrc = Kh + o2;
      asm volatile("" : "+s"(dp.vsrc), "+s"(dp.vm0), "+s"(dp.ksrc));
    } else if constexpr (part == 2) {
      dp.km0 = lds0 + kslot + w2k + (t2 & w2k);
      dp.doK = sgnmask(s - 15);
      dp.twoK = dp.doK & t2;
      const unsigned nomask = sgnmask(s - 16) & (sgnmask(s - ms1) | sgnmask(ms1 + 1 - s));
      dp.mbase = (int)(((((o0 - w2k - (t0 & w2k)) >> 8) + (t0 & (g16k >> 8)))) | nomask);
      asm volatile("" : "+s"(dp.km0), "+s"(dp.doK), "+s"(dp.twoK), "+s"(dp.mbase));
    } else {
      dp.vprev = L + ((vslot ^ (K2_VOFF ^ (K2_VOFF + 32768))) + (tm1 & g16k));
      dp.knext = L + (knslot + (t1 & g16k));
      asm volatile("" : "+v"(dp.vprev), "+v"(dp.knext));
    }
  };
#pragma unroll
  for (int t = 0; t < 4; ++t) c.o[t] = f32x16{};
  c.negm = f32x16{};
  c.l = f32x4{}; c.fpend = 1.f; c.pend = false;
  {
    const short one = (((lane >> 2) ^ (lane >> 4)) & 1) ? (short)0 : (short)0x3f80;
    c.sel = bf16x8{one, one, one, one, one, one, one, one};
    PIN(c.sel);
  }
  f32x16 SA[2], SB[2];
  int qrow = (g ? qb_hi : qb_lo) * 32 + r - 4 * hh;
  unsigned k0 = 32768, k1 = 65536, k2 = 0;
#pragma unroll
  for (int i = 0; i < KPRE; ++i) c.kf[i] = rdK(L + k0, i);
  DmaPlan dpa, dpb;
  plan_part(std::integral_constant<int, 1>{}, dpb, 0, K2_VOFF, k2, k1);
  plan_part(std::integral_constant<int, 2>{}, dpb, 0, K2_VOFF, k2, k1);
  plan_part(std::integral_constant<int, 3>{}, dpb, 0, K2_VOFF, k2, k1);
  attn_step<false>(c, SA, SB, L + k0, L, dpb.knext, qrow, true, dpb, [&](auto PT) { plan_part(PT, dpa, 1, K2_VOFF + 32768, k0, k2); });
  for (int s = 1; s < 17; s += 2) {
    WAIT_BAR(0);
    attn_step<true>(c, SB, SA, dpb.knext, dpa.vprev, dpa.knext, qrow, false, dpa,
                    [&](auto PT) { plan_part(PT, dpb, s + 1, K2_VOFF, k1, k0); });
    if (__builtin_expect(w == 0 && s == n1 + 1, 0)) {
      if (lane == 0) __hip_atomic_store(sync + SY_CTX + lo * 64 + h, tag, __ATOMIC_RELAXED, __HIP_MEMORY_SCOPE_AGENT);
    }
    const bool sw = (g == 0) && (s + 1 == n1);
    if (__builtin_expect(sw, 0)) {
#pragma unroll
      for (int f = 0; f < 8; ++f) c.q[f] = lds128(L + qoff_hi + f * 1024);
      c.negm = f32x16{};
      qrow = qb_hi * 32 + r - 4 * hh;
    }
    WAIT_BAR(0);
    attn_step<true>(c, SA, SB, dpa.knext, dpb.vprev, dpb.knext, qrow, sw, dpb,
                    [&](auto PT) { plan_part(PT, dpa, s + 2, K2_VOFF + 32768, k2, k1); });
    { const unsigned t_ = k2; k2 = k1; k1 = k0; k0 = t_; }
    if (__builtin_expect(sw, 0)) {
      const float inv = V_DN / c.l[0];
      char* dst = ctxf + ((size_t)qb_lo * 256 + h * 8) * 1024;
      st16co<0>(dst, voff, pack8<0>(c.o[0], inv)); st16co<1024>(dst, voff, pack8<1>(c.o[0], inv));
      st16co<2048>(dst, voff, pack8<0>(c.o[1], inv)); st16co<3072>(dst, voff, pack8<1>(c.o[1], inv));
      st16co<0>(dst + 4096, voff, pack8<0>(c.o[2], inv)); st16co<1024>(dst + 4096, voff, pack8<1>(c.o[2], inv));
      st16co<2048>(dst + 4096, voff, pack8<0>(c.o[3], inv)); st16co<3072>(dst + 4096, voff, pack8<1>(c.o[3], inv));
#pragma unroll
      for (int t = 0; t < 4; ++t) c.o[t] = f32x16{};
      c.l = f32x4{};
    }
  }
  {
#pragma unroll
    for (int i = 0; i < 16; ++i) SA[1][i] = EX2(SA[1][i]);
    c.p[1][2] = cvtpk(SA[0][12], SA[0][13]); c.p[1][3] = cvtpk(SA[0][14], SA[0][15]);
    c.p[2] = pack8<0>(SA[1]); c.p[3] = pack8<1>(SA[1]);
#pragma unroll
    for (int k = 0; k < 4; ++k) c.l = __builtin_amdgcn_mfma_f32_16x16x32_bf16(c.sel, __builtin_bit_cast(bf16x8, c.p[k]), c.l, 0, 0, 0);
    WAIT_BAR(0);
    if (w == 0 && n1 == 16 && lane == 0) __hip_atomic_store(sync + SY_CTX + lo * 64 + h, tag, __ATOMIC_RELAXED, __HIP_MEMORY_SCOPE_AGENT);
  }
  {
    const lds_cptr vb = L + K2_VOFF + (g ? 16384 : 0);
    const int nkb = g ? rg_hi - 1 : rg_hi + 1;
    const lds_ptr myx = (lds_ptr)smem + w * 9216;
    const lds_cptr px = (lds_cptr)smem + (7 - w) * 9216;
    const float lrow = c.l[0];
    *(__attribute__((address_space(3))) f32x2*)(myx + 8192 + lane * 8) = f32x2{c.negm[0], lrow};
    auto pv2 = [&](auto T0) {
      constexpr int t0 = decltype(T0)::value;
      if (nkb > 0) {
        const bf16x8 v0 = rdV(vb, t0), v1 = rdV(vb, t0 + 1), v2 = rdV(vb, 4 + t0), v3 = rdV(vb, 5 + t0);
        c.o[t0] = MFMA(v0, __builtin_bit_cast(bf16x8, c.p[0]), c.o[t0]); c.o[t0 + 1] = MFMA(v1, __builtin_bit_cast(bf16x8, c.p[0]), c.o[t0 + 1]);
        c.o[t0] = MFMA(v2, __builtin_bit_cast(bf16x8, c.p[1]), c.o[t0]); c.o[t0 + 1] = MFMA(v3, __builtin_bit_cast(bf16x8, c.p[1]), c.o[t0 + 1]);
      }
      if (nkb > 1) {
        const bf16x8 v0 = rdV(vb, 8 + t0), v1 = rdV(vb, 9 + t0), v2 = rdV(vb, 12 + t0), v3 = rdV(vb, 13 + t0);
        c.o[t0] = MFMA(v0, __builtin_bit_cast(bf16x8, c.p[2]), c.o[t0]); c.o[t0 + 1] = MFMA(v1, __builtin_bit_cast(bf16x8, c.p[2]), c.o[t0 + 1]);
        c.o[t0] = MFMA(v2, __builtin_bit_cast(bf16x8, c.p[3]), c.o[t0]); c.o[t0 + 1] = MFMA(v3, __builtin_bit_cast(bf16x8, c.p[3]), c.o[t0 + 1]);
      }
    };
    auto send = [&](auto T0) {
      constexpr int t0 = decltype(T0)::value;
#pragma unroll
      for (int tt = 0; tt < 2; ++tt)
#pragma unroll
        for (int qd = 0; qd < 4; ++qd)
          *(__attribute__((address_space(3))) f32x4*)(myx + (tt * 4 + qd) * 1024 + lane * 16) =
              f32x4{c.o[t0 + tt][4 * qd], c.o[t0 + tt][4 * qd + 1], c.o[t0 + tt][4 * qd + 2], c.o[t0 + tt][4 * qd + 3]};
    };
    if (g) {
      pv2(std::integral_constant<int, 0>{}); pv2(std::integral_constant<int, 2>{}); send(std::integral_constant<int, 0>{});
      c.o[0] = c.o[2]; c.o[1] = c.o[3];
    } else {
      pv2(std::integral_constant<int, 2>{}); pv2(std::integral_constant<int, 0>{}); send(std::integral_constant<int, 2>{});
    }
    LBAR();
    const f32x2 pm = *(const __attribute__((address_space(3))) f32x2*)(px + 8192 + lane * 8);
    const float nm = __builtin_fminf(c.negm[0], pm[0]);
    const float wa = EX2(nm - c.negm[0]), wb = EX2(nm - pm[0]);
    const float inv = V_DN / (lrow * wa + pm[1] * wb);
    const float fa = wa * inv, fb = wb * inv;
    char* dst = ctxf + ((size_t)qb_hi * 256 + h * 8 + (g ? 4 : 0)) * 1024;
    f32x16 o[2];
#pragma unroll
    for (int tt = 0; tt < 2; ++tt) {
#pragma unroll
      for (int qd = 0; qd < 4; ++qd) {
        const f32x4 v = *(const __attribute__((address_space(3))) f32x4*)(px + (tt * 4 + qd) * 1024 + lane * 16);
#pragma unroll
        for (int e = 0; e < 4; ++e) o[tt][4 * qd + e] = c.o[tt][4 * qd + e] * fa + v[e] * fb;
      }
    }
    st16co<0>(dst, voff, pack8<0>(o[0])); st16co<1024>(dst, voff, pack8<1>(o[0]));
    st16co<2048>(dst, voff, pack8<0>(o[1])); st16co<3072>(dst, voff, pack8<1>(o[1]));
  }
  WAIT_BAR(0);
  {
    const int qsel = idx & 3, mb64 = idx >> 2;
    const int qb64 = (qsel >> 1) * 16 + 2 * xcd + (qsel & 1);
    if (w == 0) {
      if (lane == 0) __hip_atomic_store(sync + SY_CTX + hi * 64 + h, tag, __ATOMIC_RELAXED, __HIP_MEMORY_SCOPE_AGENT);
      wait_flags(sync + SY_CTX + (qb64 >> 1) * 64, 32, tag, lane);
      if (bid == 0 && lane == 0) __hip_atomic_store(sync, tag - TAGC + 1u, __ATOMIC_RELAXED, __HIP_MEMORY_SCOPE_AGENT);
    } else if (w < 5) {
      wait_flags(sync + SY_M + (w - 1) * 64, 64, tag, lane);
    }
    WAIT_BAR(0);
    u32x4 yt;
    int ln = lane; asm volatile("" : "+v"(ln));
    asm volatile("global_load_dwordx4 %0, %1, %2 sc1" : "=v"(yt) : "v"((unsigned)((4 * (ln & 7) + (ln >> 4)) * 8192 + ((ln >> 3) & 1) * 16)),
                 "s"(yT + ((size_t)xcd * 32 * 2048 + 64 * idx + 8 * w) * 4) : "memory");
    outproj8(ctxf, Wof, P.out, smem, qb64, mb64, lane, w);
    asm volatile("" : "+v"(yt), "+v"(ln));
    *(u32x4*)((char*)(P.out + (size_t)(64 * idx + 8 * w) * 1024 + 512 + 64 * xcd) + ((4 * ((ln >> 3) & 1) + (ln >> 4)) * 2048 + (ln & 7) * 16)) = tr4x4(yt);
  }
}

extern "C" void kernel_launch(void* const* d_in, const int* in_sizes, int n_in, void* d_out, int out_size, void* d_ws,
                              size_t ws_size, hipStream_t stream) {
  Params P;
  P.x = (const u16*)d_in[0]; P.c = (const u16*)d_in[1]; P.wq = (const u16*)d_in[2]; P.wk = (const u16*)d_in[3];
  P.wv = (const u16*)d_in[4]; P.wo = (const u16*)d_in[5]; P.u = (const u16*)d_in[6]; P.Are = (const u16*)d_in[7];
  P.Aim = (const u16*)d_in[8]; P.Bre = (const u16*)d_in[9]; P.Bim = (const u16*)d_in[10]; P.Cre = (const u16*)d_in[11];
  P.Cim = (const u16*)d_in[12]; P.logdt = (const u16*)d_in[13]; P.Dskip = (const u16*)d_in[14];
  P.out = (u16*)d_out; P.ws = (char*)d_ws;
  (void)hipFuncSetAttribute((const void*)mega, hipFuncAttributeMaxDynamicSharedMemorySize, LDS_ALL);
  mega<<<256, 512, LDS_ALL, stream>>>(P);
}
```
